```python
import torch

def _softmax(x, axis=-1):
    return torch.softmax(x, dim=axis)
def _gelu(x):
    return torch.nn.functional.gelu(x, approximate="tanh")
def _silu(x):
    return torch.nn.functional.silu(x)
def _sigmoid(x):
    return torch.sigmoid(x)
def _layernorm(x, axis=-1, eps=1e-5):
    m = x.mean(dim=axis, keepdim=True)
    v = x.var(dim=axis, keepdim=True, unbiased=False)
    return (x - m) / torch.sqrt(v + eps)
def _scatter_add(tgt, idx, src, axis=0):
    return tgt.clone().index_add_(axis, idx, src)
def _segment_sum(src, seg, n):
    out = torch.zeros((n,) + src.shape[1:], dtype=src.dtype)
    return out.index_add_(0, seg, src)
def _pad(x, pw):
    flat = tuple(v for pair in reversed(pw) for v in pair)
    return torch.nn.functional.pad(x, flat)

kernel_name = 'parallel_sum__ssm_conv_s5__mixer_token_mlp__prompt'

def setup_inputs():

    _w = (lambda a: (lambda t: t.to(torch.bfloat16) if t.is_floating_point() else t)(torch.as_tensor(a)))
    def _A_setup():
        B, S, D = 1, 2048, 1024
        P = D
        Lambda_re = -0.5 * (lambda _x: torch.exp(torch.as_tensor(_x)))((lambda *s: torch.randn(*s, dtype=torch.bfloat16) * 0.1)(P))
        Lambda_im = (lambda *s: torch.randn(*s, dtype=torch.bfloat16) * 0.4)(P) * 1.0
        B_re = (lambda *s: torch.randn(*s, dtype=torch.bfloat16) * 0.2)(P, D) * (D ** -0.5)
        B_im = (lambda *s: torch.randn(*s, dtype=torch.bfloat16) * 0.2)(P, D) * (D ** -0.5)
        C_re = (lambda *s: torch.randn(*s, dtype=torch.bfloat16) * 0.2)(D, P) * (P ** -0.5)
        C_im = (lambda *s: torch.randn(*s, dtype=torch.bfloat16) * 0.2)(D, P) * (P ** -0.5)
        log_dt = (lambda *s: torch.randn(*s, dtype=torch.bfloat16) * 0.1)(P) * 0.3 - 2.0
        Dskip = (lambda *s: torch.randn(*s, dtype=torch.bfloat16) * 0.05)(D)
        return {
            "x": _w((lambda *s: torch.randn(*s, dtype=torch.bfloat16) * 0.05)(B, S, D)),
            "Lambda_re": _w(Lambda_re),
            "Lambda_im": _w(Lambda_im),
            "B_re": _w(B_re),
            "B_im": _w(B_im),
            "C_re": _w(C_re),
            "C_im": _w(C_im),
            "log_dt": _w(log_dt),
            "Dskip": _w(Dskip),
        }
    _A_in = _A_setup()

    def _B_setup():
        B, S, D, F = 1, 2048, 1024, 3584
        return {
            "x": _w((lambda *s: torch.randn(*s, dtype=torch.bfloat16) * 0.05)(B, S, D)),
            "w1": _w((lambda *s: torch.randn(*s, dtype=torch.bfloat16) * 0.8)(S, F) * (S ** -0.5)),
            "b1": _w((lambda *s: torch.randn(*s, dtype=torch.bfloat16) * 3.2)(F) * 0.02),
            "w2": _w((lambda *s: torch.randn(*s, dtype=torch.bfloat16) * 0.4)(F, S) * (F ** -0.5)),
            "b2": _w((lambda *s: torch.randn(*s, dtype=torch.bfloat16) * 1.6)(S) * 0.02),
        }
    _B_in = _B_setup()

    return {
        "A_x": _A_in["x"],
        "A_Lambda_re": _A_in["Lambda_re"],
        "A_Lambda_im": _A_in["Lambda_im"],
        "A_B_re": _A_in["B_re"],
        "A_B_im": _A_in["B_im"],
        "A_C_re": _A_in["C_re"],
        "A_C_im": _A_in["C_im"],
        "A_log_dt": _A_in["log_dt"],
        "A_Dskip": _A_in["Dskip"],
        "B_x": _B_in["x"],
        "B_w1": _B_in["w1"],
        "B_b1": _B_in["b1"],
        "B_w2": _B_in["w2"],
        "B_b2": _B_in["b2"],
    }

def reference(A_x, A_Lambda_re, A_Lambda_im, A_B_re, A_B_im, A_C_re, A_C_im, A_log_dt, A_Dskip, B_x, B_w1, B_b1, B_w2, B_b2):
    def _A_ref(x, Lambda_re, Lambda_im, B_re, B_im, C_re, C_im, log_dt, Dskip):
        Bsz, L, H = x.shape
        Lam = Lambda_re + 1j * Lambda_im
        Bmat = B_re + 1j * B_im
        Cmat = C_re + 1j * C_im
        dt = torch.exp(log_dt)
        Lam_bar = torch.exp(dt * Lam)
        B_bar = ((Lam_bar - 1.0) / Lam)[:, None] * Bmat
        Bu = torch.einsum("bld,pd->blp", x + 0j, B_bar)
        h = torch.zeros_like(Bu[:, 0, :])
        hs = []
        for t in range(L):
            h = Lam_bar[None, :] * h + Bu[:, t, :]
            hs.append(h)
        hseq = torch.stack(hs, 1)
        y = 2.0 * torch.einsum("blp,dp->bld", hseq, Cmat).real + x * Dskip[None, None, :]
        return y

    def _B_ref(x, w1, b1, w2, b2):
        B, S, D = x.shape
        h = _layernorm(x, axis=-1)
        u = torch.einsum("bsd,sf->bdf", h, w1) + b1
        u = _gelu(u)
        y = torch.einsum("bdf,fs->bds", u, w2) + b2
        return (lambda x, *d: x.permute(*d))(y, 0, 2, 1)

    x = A_x
    xn = _layernorm(x, axis=-1)
    return x + _A_ref(xn, A_Lambda_re, A_Lambda_im, A_B_re, A_B_im, A_C_re, A_C_im, A_log_dt, A_Dskip) + _B_ref(xn, B_w1, B_b1, B_w2, B_b2)
```

```cpp
#include <hip/hip_runtime.h>
#include <hip/hip_bf16.h>
typedef unsigned short u16;
__device__ __forceinline__ float bf2f(u16 v){ return __uint_as_float(((unsigned)v)<<16); }
__device__ __forceinline__ u16 f2bf(float f){ unsigned u=__float_as_uint(f); u += 0x7FFFu + ((u>>16)&1u); return (u16)(u>>16); }
__device__ __forceinline__ float rbf(float f){ return bf2f(f2bf(f)); }

constexpr int S=2048, D=1024, P=1024, F=3584;

__global__ void k_ln(const u16* x, u16* xn, u16* h){
  int r=blockIdx.x, t=threadIdx.x; __shared__ float red[256];
  float v[4];
  for(int i=0;i<4;i++) v[i]=bf2f(x[r*D+t*4+i]);
  for(int pass=0;pass<2;pass++){
    float s=v[0]+v[1]+v[2]+v[3]; red[t]=s; __syncthreads();
    for(int o=128;o>0;o>>=1){ if(t<o) red[t]+=red[t+o]; __syncthreads(); }
    float mean=red[0]/D; __syncthreads();
    float q=0; for(int i=0;i<4;i++){ float d=v[i]-mean; q+=d*d; }
    red[t]=q; __syncthreads();
    for(int o=128;o>0;o>>=1){ if(t<o) red[t]+=red[t+o]; __syncthreads(); }
    float var=red[0]/D; __syncthreads();
    float mb=rbf(mean), vb=rbf(var);
    float sd=rbf(sqrtf(rbf(vb+1e-5f)));
    for(int i=0;i<4;i++) v[i]=rbf(rbf(v[i]-mb)/sd);
    u16* o = pass==0? xn : h;
    for(int i=0;i<4;i++) o[r*D+t*4+i]=f2bf(v[i]);
  }
}
__global__ void k_gemm(const u16* A, long sam, long sak, const u16* B, long sbn, long sbk, float* C, int M, int N, int K){
  __shared__ float As[16][17], Bs[16][17];
  int tx=threadIdx.x, ty=threadIdx.y; int m=blockIdx.y*16+ty, n=blockIdx.x*16+tx; float acc=0;
  for(int k0=0;k0<K;k0+=16){
    As[ty][tx]=bf2f(A[(long)(blockIdx.y*16+ty)*sam+(long)(k0+tx)*sak]);
    Bs[ty][tx]=bf2f(B[(long)(blockIdx.x*16+ty)*sbn+(long)(k0+tx)*sbk]);
    __syncthreads();
    for(int k=0;k<16;k++) acc+=As[ty][k]*Bs[tx][k];
    __syncthreads();
  }
  C[(long)m*N+n]=acc;
}
__global__ void k_scan(const float* bre, const float* bim, const u16* lre,const u16* lim,const u16* ldt, u16* hre, u16* him){
  int p=blockIdx.x*blockDim.x+threadIdx.x; if(p>=P) return;
  float lr=bf2f(lre[p]), li=bf2f(lim[p]); float dt=rbf(expf(bf2f(ldt[p])));
  float er=expf(dt*lr); float ar=er*cosf(dt*li), ai=er*sinf(dt*li);
  float nr=ar-1.f, ni=ai; float den=lr*lr+li*li; float cr=(nr*lr+ni*li)/den, ci=(ni*lr-nr*li)/den;
  float hr=0,hi=0;
  for(int t=0;t<S;t++){
    float ur=bre[t*P+p], ui=bim[t*P+p];
    float br=cr*ur-ci*ui, bi=cr*ui+ci*ur;
    float nhr=ar*hr-ai*hi+br, nhi=ar*hi+ai*hr+bi; hr=nhr; hi=nhi;
    hre[t*P+p]=f2bf(hr); him[t*P+p]=f2bf(hi);
  }
}
__global__ void k_gelu(const float* u, const u16* b1, u16* g){
  long i=(long)blockIdx.x*256+threadIdx.x; int f=i%F; float v=rbf(rbf(u[i])+bf2f(b1[f]));
  float t=tanhf(0.7978845608028654f*(v+0.044715f*v*v*v)); g[i]=f2bf(0.5f*v*(1.f+t));
}
__global__ void k_final(const u16* x,const u16* xn,const u16* dsk,const float* yr,const float* yi,const float* yb,const u16* b2,float* out){
  long i=(long)blockIdx.x*256+threadIdx.x; int s=i/D, d=i%D;
  float a=2.f*(yr[i]-yi[i])+rbf(bf2f(xn[i])*bf2f(dsk[d]));
  float b=rbf(rbf(yb[i])+bf2f(b2[s]));
  out[i]=bf2f(x[i])+a+b;
}
extern "C" void kernel_launch(void* const* d_in, const int* in_sizes, int n_in, void* d_out, int out_size, void* d_ws, size_t ws_size, hipStream_t stream){
  const u16* x=(const u16*)d_in[0]; const u16* lre=(const u16*)d_in[1]; const u16* lim=(const u16*)d_in[2];
  const u16* Bre=(const u16*)d_in[3]; const u16* Bim=(const u16*)d_in[4]; const u16* Cre=(const u16*)d_in[5]; const u16* Cim=(const u16*)d_in[6];
  const u16* ldt=(const u16*)d_in[7]; const u16* dsk=(const u16*)d_in[8];
  const u16* w1=(const u16*)d_in[10]; const u16* b1=(const u16*)d_in[11]; const u16* w2=(const u16*)d_in[12]; const u16* b2=(const u16*)d_in[13];
  char* ws=(char*)d_ws; size_t o=0;
  auto al=[&](size_t n){ char* p=ws+o; o+=(n+255)&~(size_t)255; return p; };
  u16* xn=(u16*)al(S*D*2); u16* h=(u16*)al(S*D*2);
  float* bre=(float*)al((size_t)S*P*4); float* bim=(float*)al((size_t)S*P*4);
  u16* hre=(u16*)al(S*P*2); u16* him=(u16*)al(S*P*2);
  float* u=(float*)al((size_t)D*F*4); u16* g=(u16*)al((size_t)D*F*2);
  float* yr=(float*)al((size_t)S*D*4); float* yi=(float*)al((size_t)S*D*4); float* yb=(float*)al((size_t)S*D*4);
  k_ln<<<S,256,0,stream>>>(x,xn,h);
  dim3 tb(16,16);
  k_gemm<<<dim3(P/16,S/16),tb,0,stream>>>(xn,D,1,Bre,D,1,bre,S,P,D);
  k_gemm<<<dim3(P/16,S/16),tb,0,stream>>>(xn,D,1,Bim,D,1,bim,S,P,D);
  k_scan<<<P/64,64,0,stream>>>(bre,bim,lre,lim,ldt,hre,him);
  k_gemm<<<dim3(D/16,S/16),tb,0,stream>>>(hre,P,1,Cre,P,1,yr,S,D,P);
  k_gemm<<<dim3(D/16,S/16),tb,0,stream>>>(him,P,1,Cim,P,1,yi,S,D,P);
  k_gemm<<<dim3(F/16,D/16),tb,0,stream>>>(h,1,D,w1,1,F,u,D,F,S);
  k_gelu<<<D*F/256,256,0,stream>>>(u,b1,g);
  k_gemm<<<dim3(D/16,S/16),tb,0,stream>>>(w2,1,S,g,F,1,yb,S,D,F);
  k_final<<<S*D/256,256,0,stream>>>(x,xn,dsk,yr,yi,yb,b2,(float*)d_out);
}
```

```cpp
#include <hip/hip_runtime.h>
#include <hip/hip_bf16.h>

typedef unsigned short u16;
typedef unsigned u32x4 __attribute__((ext_vector_type(4)));
typedef unsigned u32x2 __attribute__((ext_vector_type(2)));
typedef __bf16 bf16x4 __attribute__((ext_vector_type(4)));
typedef __bf16 bf16x2 __attribute__((ext_vector_type(2)));
typedef float f32x4 __attribute__((ext_vector_type(4)));
typedef float f32x2 __attribute__((ext_vector_type(2)));
typedef unsigned long long u64;
#define LAS __attribute__((address_space(3)))
#define GAS __attribute__((address_space(1)))
#define DEVI __device__ __forceinline__

constexpr int S = 2048, D = 1024, P = 1024, F = 3584;
constexpr int LDH = D + 128, LDS_ = S + 128, LDG = F + 128;

DEVI unsigned pk_bf16(float lo, float hi) {
  bf16x2 r; r.x = (__bf16)lo; r.y = (__bf16)hi; return __builtin_bit_cast(unsigned, r);
}
DEVI float rbf(float f) { return (float)(__bf16)f; }
DEVI int lane_id() { int l; asm volatile("v_mbcnt_lo_u32_b32 %0, -1, 0\n\tv_mbcnt_hi_u32_b32 %0, -1, %0" : "=v"(l)); return l; }

DEVI u32x4 make_rsrc(const void* p) {
  unsigned long long a = (unsigned long long)p;
  u32x4 r; r.x = __builtin_amdgcn_readfirstlane((unsigned)a);
  r.y = __builtin_amdgcn_readfirstlane((unsigned)(a >> 32)) & 0xffffu; r.z = 0x7fffffffu; r.w = 0x00020000u; return r;
}
DEVI void dma16(u32x4 rsrc, unsigned voff, unsigned soff, unsigned ldsaddr) {
  asm volatile("s_mov_b32 m0, %3\n\ts_nop 3\n\tbuffer_load_dwordx4 %0, %1, %2 offen lds"
               :: "v"(voff), "s"(rsrc), "s"(soff), "s"(ldsaddr) : "memory");
}
DEVI void dma16c(u32x4 rsrc, unsigned voff, unsigned soff, unsigned ldsaddr) {
  asm volatile("s_mov_b32 m0, %3\n\ts_nop 3\n\tbuffer_load_dwordx4 %0, %1, %2 offen sc1 lds"
               :: "v"(voff), "s"(rsrc), "s"(soff), "s"(ldsaddr) : "memory");
}
#define WAITV(n) asm volatile("s_waitcnt vmcnt(%0)" ::"n"(n) : "memory")
#define WAITL0() asm volatile("s_waitcnt lgkmcnt(0)" ::: "memory")
template <int N> DEVI void waitl() { __builtin_amdgcn_s_waitcnt(0xC07F | (N << 8)); }
#define SCHED() __builtin_amdgcn_sched_barrier(0)
#define BAR() do { SCHED(); asm volatile("s_barrier" ::: "memory"); SCHED(); } while (0)

DEVI unsigned swz_kc(int lane, int par) { int r8 = lane >> 3, c = lane & 7; return (unsigned)((c ^ ((4 * par + (r8 >> 1)) & 7)) << 4); }
DEVI unsigned swz_km(int lane, int par) { int r8 = lane >> 3, c = lane & 7; int s = ((r8 >> 1) & 1) | (par << 1); return (unsigned)((c ^ (s << 1)) << 4); }
DEVI unsigned frag_base_kc(int lane, int ks) { int r = lane & 15, q = lane >> 4; return r * 128 + ((((4 * ks + q) ^ (r >> 1)) & 7) << 4); }
DEVI unsigned frag_base_km(int lane, int c2) { int g = lane >> 4, q = (lane & 15) >> 2, p = lane & 3; int s = ((q >> 1) & 1) | ((g & 1) << 1); return (8 * g + q) * 128 + ((c2 ^ s) << 5) + 8 * p; }

template <int KM, int NWIN> struct Side {
  unsigned a[NWIN][KM == 1 ? 4 : 2];
  DEVI void init(int lane, int blk0, unsigned img) {
#pragma unroll
    for (int i = 0; i < (KM == 1 ? 4 : 2); i++) {
      unsigned b = KM ? img + frag_base_km(lane, (blk0 + i) & 3) + ((blk0 + i) >> 2) * 8192 : img + frag_base_kc(lane, i) + blk0 * 2048;
#pragma unroll
      for (int w = 0; w < NWIN; w++) { a[w][i] = b + w * 65536; asm volatile("" : "+v"(a[w][i])); }
    }
  }
  static constexpr int NI = KM ? 2 : 1;
  DEVI void rd1(u32x4& f, int part, int blk, int h, int sbase) const {
    if constexpr (KM == 0) { const int c = sbase + blk * 2048; f = *(LAS u32x4*)(unsigned long)(a[c >> 16][h] + (c & 65535)); }
    else {
      const int c = sbase + (KM == 1 ? blk >> 2 : blk >> 1) * 8192 + h * 4096;
      const u32x2 v = __builtin_bit_cast(u32x2, __builtin_amdgcn_ds_read_tr16_b64_v4bf16((LAS bf16x4*)(unsigned long)(a[c >> 16][blk & (KM == 1 ? 3 : 1)] + (c & 65535) + part * 512)));
      if (part == 0) { f.x = v.x; f.y = v.y; } else { f.z = v.x; f.w = v.y; }
    }
  }
};

constexpr int STG = 384 * 128, NS = 3, PW = 12;
template <class StageFn, class HookFn, class GateFn>
DEVI void producer_steady(int k0, int NT, StageFn&& stage, HookFn&& hook, int GATE, GateFn&& gate) {
  int extra = 0, issued = k0 + NS, fb = k0;
#pragma unroll 1
  for (int k = k0; k < NT - 1; k++) {
    if (issued > k + 2) { WAITV(PW); }
    else if (extra >= 5) { WAITV(5); } else if (extra == 4) { WAITV(4); } else if (extra == 3) { WAITV(3); } else if (extra == 2) { WAITV(2); } else if (extra == 1) { WAITV(1); } else { WAITV(0); }
    BAR();
#pragma unroll 1
    while (issued <= k + NS && issued < NT) {
      if (issued == GATE && !gate(issued == k + 2)) break;
      stage(issued, fb, 0, PW); issued++; fb = fb == NS - 1 ? 0 : fb + 1;
    }
    extra += hook(k);
  }
}
template <class StageFn, class HookFn, class GateFn> DEVI void producer_loop(int NT, StageFn&& stage, HookFn&& hook, int GATE, GateFn&& gate) {
  stage(0, 0, 0, PW);
  stage(1, 1, 0, PW / 2);
  WAITV(PW / 2); BAR();
  stage(1, 1, PW / 2, PW);
  stage(2, 2, 0, PW);
  producer_steady(0, NT, stage, hook, GATE, gate);
}
template <int RKM, int CKM, int MB, int NB, int REM>
DEVI void consumer_loop(f32x4 (&acc)[MB][NB], int NFULL, int lane, int rblk0, int cblk0, unsigned cimg) {
  constexpr int NWIN = (NS * STG + 65535) / 65536;
  Side<RKM, NWIN> sR; sR.init(lane, rblk0, 0);
  Side<CKM, NWIN> sC; sC.init(lane, cblk0, cimg);
  constexpr bool RL = MB >= NB;
  constexpr int LB = RL ? MB : NB, PB = LB == 8 ? 6 : 4;
  static_assert((RL ? NB : MB) == 4 && (LB == 7 || LB == 8), "");
  u32x4 fS[4][2], fN[4][2], fL[LB][2];
  constexpr int LI = RL ? sR.NI : sC.NI, SI = RL ? sC.NI : sR.NI;
  auto rdL = [&](int l, int buf, int i) { if constexpr (RL) sR.rd1(fL[l][i / LI], i % LI, l, i / LI, buf * STG); else sC.rd1(fL[l][i / LI], i % LI, l, i / LI, buf * STG); };
  auto rdN = [&](int t, int buf, int i) { if constexpr (RL) sC.rd1(fN[t][i / SI], i % SI, t, i / SI, buf * STG); else sR.rd1(fN[t][i / SI], i % SI, t, i / SI, buf * STG); };
  auto rdg = [&](int g, int c0, int c1, int c2, int buf, int nbuf) {
    const int cs[3] = {c0, c1, c2};
#pragma unroll
    for (int k = 0; k < 3; k++) {
      const int c = cs[k], n = c < 0 ? 0 : c < 16 ? 2 * LI : 2 * SI;
      if (g >= 0 && g < n) { if (c < 8) rdL(c, buf, g); else if (c < 16) rdL(c - 8, nbuf, g); else rdN(c - 16, nbuf, g); }
      g -= n;
    }
  };
  auto step = [&](int buf, int nbuf) {
#pragma unroll
    for (int t = 0; t < 4; t++) { fS[t][0] = fN[t][0]; fS[t][1] = fN[t][1]; }
#pragma unroll
    for (int l = 0; l < LB; l++) {
      int c0 = -1, c1 = -1, c2 = -1;
      if (LB == 8) {
        if (l == 0) { c0 = 2; c1 = 3; } else if (l <= 4) c0 = l + 3;
        else if (nbuf >= 0 && l == 6) { c0 = 8; c1 = 16; c2 = 17; } else if (nbuf >= 0 && l == 7) { c0 = 18; c1 = 19; c2 = 9; }
      } else {
        if (l <= 1) { c0 = 2 * l + 2; c1 = 2 * l + 3; } else if (l == 2) c0 = 6;
        else if (nbuf >= 0 && l == 4) { c0 = 8; c1 = 16; } else if (nbuf >= 0 && l == 5) { c0 = 17; c1 = 18; } else if (nbuf >= 0 && l == 6) { c0 = 19; c1 = 9; }
      }
      constexpr int BL = 2 * LI, BS = 2 * SI;
      const int cnt = (c0 < 0 ? 0 : c0 < 16 ? BL : BS) + (c1 < 0 ? 0 : c1 < 16 ? BL : BS) + (c2 < 0 ? 0 : c2 < 16 ? BL : BS);
#pragma unroll
      for (int g = 0; g < 8; g++) {
        const int t = g >> 1, h = (g ^ t) & 1;
        SCHED();
        if (g == 0 && l == 0) waitl<LB == 8 ? 2 * BS + BL : BS + BL>();
        else if (g == 4 && l == 0) waitl<BL + (2 * BL <= 4 ? 2 : 4)>();
        else if (g == 0 && l < PB) { if (LB == 8 || l == 1) waitl<2 * BL>(); else waitl<3 * BL>(); }
        if constexpr (RL) asm volatile("v_mfma_f32_16x16x32_bf16 %0, %1, %2, %0" : "+v"(acc[l][t]) : "v"(fL[l][h]), "v"(fS[t][h]));
        else asm volatile("v_mfma_f32_16x16x32_bf16 %0, %1, %2, %0" : "+v"(acc[t][l]) : "v"(fS[t][h]), "v"(fL[l][h]));
        SCHED();
        if (cnt > 4) rdg(g, c0, c1, c2, buf, nbuf); else if ((g & 1) == 0) rdg(t, c0, c1, c2, buf, nbuf);
      }
      if (l == PB - 1) { SCHED(); waitl<0>(); if (nbuf >= 0) BAR(); }
    }
    SCHED();
  };
#pragma unroll
  for (int m = 0; m < MB; m++)
#pragma unroll
    for (int n = 0; n < NB; n++) asm volatile("" : "+v"(acc[m][n]));
  waitl<0>();
  __builtin_amdgcn_s_setprio(1);
  BAR();
#pragma unroll
  for (int g = 0; g < 12; g++) { rdg(g, 8, 16, 17, 0, 0); SCHED(); }
#pragma unroll
  for (int g = 0; g < 12; g++) { rdg(g, 18, 19, 9, 0, 0); SCHED(); }
#pragma unroll 1
  for (int it = 0; it < NFULL; it++) {
#pragma unroll
    for (int u = 0; u < NS; u++) step(u, (u + 1) % NS);
  }
#pragma unroll
  for (int u = 0; u < REM; u++) step(u, (u + 1) % NS);
  step(REM, -1);
  asm volatile("s_nop 7\n\ts_nop 1");
  SCHED();
  __builtin_amdgcn_s_setprio(0);
}

DEVI void dma4c(u32x4 rsrc, unsigned voff, unsigned soff, unsigned ldsaddr) {
  asm volatile("s_mov_b32 m0, %3\n\ts_nop 3\n\tbuffer_load_dword %0, %1, %2 offen sc1 lds"
               :: "v"(voff), "s"(rsrc), "s"(soff), "s"(ldsaddr) : "memory");
}
DEVI void touch(u32x4 rsrc, unsigned voff, unsigned soff, unsigned ldsaddr) {
  asm volatile("s_mov_b32 m0, %3\n\ts_nop 3\n\tbuffer_load_dword %0, %1, %2 offen lds"
               :: "v"(voff), "s"(rsrc), "s"(soff), "s"(ldsaddr) : "memory");
}

struct Ptrs {
  const u16 *x, *lre, *lim, *Bre, *Bim, *Cre, *Cim, *ldt, *dsk, *w1, *b1, *w2, *b2;
  char* ws; float* out;
  DEVI u16* h_() const { return (u16*)ws; }
  DEVI u16* hsT_() const { return (u16*)(ws + (6u << 20)); }
  DEVI u16* g_() const { return (u16*)(ws + (16u << 20)); }
  DEVI char* xch_() const { return ws + (24u << 20); }
  DEVI u64* gran_() const { return (u64*)(ws + (40u << 20)); }
  DEVI u64* stat_() const { return (u64*)(ws + (40u << 20) + (128u << 10)); }
  DEVI u64* sb_() const { return (u64*)(ws + (40u << 20) + (160u << 10)); }
  DEVI unsigned* flag_() const { return (unsigned*)(ws + (40u << 20) + (192u << 10)); }
  DEVI unsigned* gfl_() const { return (unsigned*)(ws + (40u << 20) + (208u << 10)); }
  DEVI unsigned* lnf_() const { return (unsigned*)(ws + (40u << 20) + (212u << 10)); }
  DEVI unsigned* epo_() const { return (unsigned*)(ws + (40u << 20) + (216u << 10)); }
};
#define RLX_AGENT __ATOMIC_RELAXED, __HIP_MEMORY_SCOPE_AGENT
DEVI void st_flag(unsigned* w, unsigned ep) { __hip_atomic_store((GAS unsigned*)w, ep, RLX_AGENT); }
DEVI void st_gran(u64* w, unsigned ep, unsigned val) { __hip_atomic_store((GAS u64*)w, ((u64)ep << 32) | val, RLX_AGENT); }

template <int CTRL> DEVI float dppmov(float v) { return __builtin_bit_cast(float, __builtin_amdgcn_update_dpp(0, __builtin_bit_cast(int, v), CTRL, 0xf, 0xf, true)); }
DEVI float wave_sum(float v) {
  v += dppmov<0xB1>(v); v += dppmov<0x4E>(v); v += dppmov<0x141>(v); v += dppmov<0x140>(v);
  const int iv = __builtin_bit_cast(int, v);
  const float a = __builtin_bit_cast(float, __builtin_amdgcn_readlane(iv, 0)), b = __builtin_bit_cast(float, __builtin_amdgcn_readlane(iv, 16));
  const float c = __builtin_bit_cast(float, __builtin_amdgcn_readlane(iv, 32)), d = __builtin_bit_cast(float, __builtin_amdgcn_readlane(iv, 48));
  return (a + b) + (c + d);
}
DEVI float hi2f(unsigned v) { return __uint_as_float(v & 0xffff0000u); }
DEVI float lo2f(unsigned v) { return __uint_as_float(v << 16); }
DEVI f32x2 up2(unsigned v) { return (f32x2){lo2f(v), hi2f(v)}; }
DEVI float dot2(unsigned a, unsigned b, float c) { return __builtin_amdgcn_fdot2_f32_bf16(__builtin_bit_cast(bf16x2, a), __builtin_bit_cast(bf16x2, b), c, false); }
DEVI float ln_std(float var) { return rbf(__builtin_amdgcn_sqrtf(rbf(rbf(var) + 1e-5f))); }
DEVI unsigned cvt2(f32x2 v) { unsigned r; asm("v_cvt_pk_bf16_f32 %0, %1, %2" : "=v"(r) : "v"(v.x), "v"(v.y)); return r; }
template <int NR> DEVI void ln2_rows(unsigned (&w)[NR][8], unsigned (&st)[NR]) {
  constexpr unsigned ONE = 0x3f803f80u;
  float mean[NR], mb[NR];
  f32x2 d[NR][8];
#pragma unroll
  for (int r = 0; r < NR; r++) {
    float s0 = 0.f, s1 = 0.f;
#pragma unroll
    for (int i = 0; i < 4; i++) { s0 = dot2(w[r][i], ONE, s0); s1 = dot2(w[r][4 + i], ONE, s1); }
    mean[r] = wave_sum(s0 + s1) * (1.0f / D); mb[r] = rbf(mean[r]);
  }
#pragma unroll
  for (int r = 0; r < NR; r++) {
    f32x2 a = {0.f, 0.f};
#pragma unroll
    for (int i = 0; i < 8; i++) { d[r][i] = up2(w[r][i]) - mb[r]; a += d[r][i] * d[r][i]; }
    const float dm = mean[r] - mb[r], var = fmaxf(wave_sum(a.x + a.y) * (1.0f / D) - dm * dm, 0.f);
    const float sd = ln_std(var), rs = __builtin_amdgcn_rcpf(sd);
    st[r] = pk_bf16(mb[r], sd);
    const float rs2 = __builtin_amdgcn_rcpf(ln_std(var * rs * rs)), sc = rs * rs2, off = -rbf(dm * rs) * rs2;
#pragma unroll
    for (int i = 0; i < 8; i++) w[r][i] = cvt2(d[r][i] * sc + off);
  }
}

struct cf { float r, i; };
DEVI cf cmul(cf a, cf b) { return cf{a.r * b.r - a.i * b.i, a.r * b.i + a.i * b.r}; }
DEVI cf cfma(cf a, cf b, cf c) { return cf{fmaf(a.r, b.r, fmaf(-a.i, b.i, c.r)), fmaf(a.r, b.i, fmaf(a.i, b.r, c.i))}; }
DEVI f32x2 gelu2(f32x2 v) {
  const f32x2 a = v * ((v * v) * (0.044715f * 2.302208198f) + 2.302208198f);
  f32x2 d; d.x = __builtin_amdgcn_exp2f(a.x); d.y = __builtin_amdgcn_exp2f(a.y);
  d = d + 1.0f;
  f32x2 r; r.x = __builtin_amdgcn_rcpf(d.x); r.y = __builtin_amdgcn_rcpf(d.y);
  return v - v * r;
}
constexpr int GP = 464, HP = 528;
constexpr int NXA = 13;
constexpr unsigned CODE_WARM = 61440;
constexpr int NYB = 28 - NXA;
constexpr unsigned L_T = NS * STG, L_CF = L_T  , L_RAW = L_T + 4096  , L_RS = L_T + 8192  , L_SB = L_T + 10240  ,
                   L_AG = L_T + 10752  , L_TIN = L_T + 11264  , L_DUMMY = L_T + 11776  , L_EP = L_T + 12032  , L_DUM2 = L_T + 13056  , L_TOT = L_T + 14080;
constexpr unsigned SPIN = 1u << 16;
DEVI int w2_chunk(int i) {
  if (i < 128 * NXA) { const int pt = (i >> 5) & 3; return ((pt < 2 ? pt * NXA : 2 * NXA + (pt - 2) * NYB) + (i >> 7)) * 32 + (i & 31); }
  i -= 128 * NXA; return (2 * NXA + ((i >> 5) & 1) * NYB + NXA + (i >> 6)) * 32 + (i & 31);
}
DEVI bool wait_words(const unsigned* a, unsigned ep, unsigned tries = 0x10000u) {
  u32x2 v;
  asm volatile("WW_%=:\n\t"
               "global_load_dwordx2 %0, %2, off sc1\n\t"
               "s_waitcnt vmcnt(0)\n\t"
               "v_cmp_ne_u64 vcc, %3, %0\n\t"
               "s_nop 1\n\t"
               "s_cmp_eq_u64 vcc, 0\n\t"
               "s_cbranch_scc1 WWE_%=\n\t"
               "s_sub_u32 %1, %1, 1\n\t"
               "s_cmp_eq_u32 %1, 0\n\t"
               "s_cbranch_scc1 WWE_%=\n\t"
               "s_sleep 4\n\t"
               "s_branch WW_%=\n"
               "WWE_%=:"
               : "=&v"(v), "+s"(tries) : "v"(a), "s"(((u64)ep << 32) | ep) : "vcc", "scc", "memory");
  return tries != 0u;
}
DEVI void warm_code(unsigned long long pc0, int piece, int lane) {
  u32x4 rk = make_rsrc((const void*)(pc0 & ~127ull)); rk.z = CODE_WARM;
  dma16(rk, lane * 16 + piece * 1024, 0, L_DUM2);
}
DEVI unsigned lds_epoch() { return __builtin_amdgcn_readfirstlane(*(const LAS unsigned*)(unsigned long)L_EP) + 1u; }

template <int RB> DEVI void phase2(const Ptrs& p, int wid, int j, int xcd, unsigned ep) {
  constexpr int NT = RB ? NYB : 16 + NXA;
  const int grp = wid >> 2, w4 = wid & 3, wr = w4 >> 1, wc = w4 & 1;
  const int pa = xcd & 1, part = 2 * RB + pa;
  const int ts = (xcd >> 2) * 4 + (j >> 2), td = ((xcd >> 1) & 1) * 4 + (j & 3);
  const int s0 = ts * 256, d0 = td * 128, tile = ts * 8 + td;
  constexpr unsigned EB = (NT % NS) * STG;
  if (grp == 1) {
    const int lane = lane_id();
    const unsigned ldH = LDS_ * 2, ldW = S * 2, ldc = P * 2, ldg = LDG * 2;
    const int kf0 = RB ? 2 * NXA + pa * NYB : pa * NXA - 16;
    const u32x4 rCm = make_rsrc((pa ? p.Cim : p.Cre) + (size_t)d0 * P), rG = make_rsrc(p.g_() + (size_t)d0 * LDG);
    const u32x4 rH = make_rsrc(p.hsT_() + (size_t)pa * P * LDS_ + s0), rW = make_rsrc(p.w2 + s0);
    const int r8 = 8 * w4 + (lane >> 3);
    const unsigned skc = swz_kc(lane, w4 & 1), skm = swz_km(lane, w4 & 1);
    const unsigned vRc = r8 * ldc + skc, vRg = r8 * ldg + skc, vH = r8 * ldH + skm, vW = r8 * ldW + skm;
    const int u0 = (kf0 + (RB ? 0 : 16)) / 7, u1 = (kf0 + NT - 1) / 7;
    const unsigned* gw = p.gfl_() + td * 16 + 2 * min(u0 + (lane & 7), u1);
    const int kn = 1 + j % 3, ch = 768 + (xcd * 16 + j) * 4 + w4;
    auto stage = [&](int kt, int buf, int i0, int i1) {
      const unsigned lb = buf * STG + w4 * 1024;
      if (!RB && kt < 16) {
#pragma unroll
        for (int i = i0; i < i1; i++) {
          if (i < 4) dma16(rCm, vRc, kt * 128 + 32 * i * ldc, lb + i * 4096);
          else dma16c(rH, vH, kt * 64 * ldH + ((i >> 1) - 2) * 128 + 32 * (i & 1) * ldH, lb + 16384 + ((((i >> 1) - 2 - part) & 3) * 8192 + (i & 1) * 4096));
        }
      } else {
        const int kf = kf0 + kt;
#pragma unroll
        for (int i = i0; i < i1; i++) {
          if (i < 4) dma16c(rG, vRg, kf * 128 + 32 * i * ldg, lb + i * 4096);
          else dma16(rW, vW, kf * 64 * ldW + ((i >> 1) - 2) * 128 + 32 * (i & 1) * ldW, lb + 16384 + ((((i >> 1) - 2 - part) & 3) * 8192 + (i & 1) * 4096));
        }
      }
    };
    constexpr int W0 = RB ? 4 : 0, W1 = RB ? 12 : 4, D0 = RB ? 0 : 4, D1 = RB ? 4 : 12;
    if (!RB) touch(make_rsrc(p.w2), lane * 128, w2_chunk(ch) * 8192, L_DUMMY);
    stage(0, 0, W0, W1);
    stage(1, 1, W0, W1);
    if (w4 == 0) wait_words(RB ? gw : p.gfl_() + 128 + ts * 16 + 2 * (lane & 7), ep);
    BAR();
    stage(0, 0, D0, D1);
    stage(1, 1, D0, D1);
    WAITV(D1 - D0); BAR();
    stage(2, 2, 0, PW);
    producer_steady(0, NT, stage, [&](int k) {
      if (!RB && k == kn) touch(make_rsrc(p.w2), lane * 128, w2_chunk(ch + 512) * 8192, L_DUMMY);
      if (k != NT - 3) return 0;
      const u32x4 rX = make_rsrc(p.x + (size_t)(s0 + 64 * part) * D + d0);
#pragma unroll
      for (int i = 0; i < 4; i++) dma16(rX, vRc, 32 * (i & 1) * ldc + (i >> 1) * 128, EB + w4 * 1024 + i * 4096);
      dma4c(make_rsrc(w4 == 0 ? (const void*)(p.dsk + d0) : w4 == 1 ? (const void*)(p.b2 + s0 + 64 * part) : (const void*)(p.stat_() + s0 + 64 * part)),
            w4 == 0 ? lane * 4 : w4 == 1 ? (lane & 31) * 4 : lane * 8, 0, EB + 16384 + w4 * 256);
      return 5;
    }, RB ? -1 : 16, [&](bool blocking) {
      const bool ok = wait_words(gw, ep, blocking ? 0x10000u : 1u) || blocking;
      if (ok && tile == 56 && pa == 0 && w4 == 0 && lane == 0) st_flag(p.epo_(), ep);
      return ok;
    });
    WAITV(0);
    BAR();
    if (w4 == 0) {
      if (lane == 0) st_flag(p.flag_() + tile * 32 + part, ep);
      wait_words(p.flag_() + tile * 32 + 2 * (lane & 1), ep);
    }
    BAR();
  } else {
    f32x4 acc[4][8];
#pragma unroll
    for (int m = 0; m < 4; m++)
#pragma unroll
      for (int n = 0; n < 8; n++) acc[m][n] = (f32x4){0.f, 0.f, 0.f, 0.f};
    BAR();
    consumer_loop<0, 2, 4, 8, (NT - 1) % NS>(acc, (NT - 1) / NS, lane_id(), wr * 4, wc * 2, 128 * 128);
    SCHED();
    const int ln = lane_id(), q = ln >> 4, fr = ln & 15;
    const __amdgpu_buffer_rsrc_t rx = __builtin_amdgcn_make_buffer_rsrc(p.xch_() + (size_t)tile * (12 * 16384), 0, 0x7fffffff, 0x00020000);
    const unsigned lo = w4 * 4096 + ln * 16;
#pragma unroll
    for (int t = 1; t < 4; t++)
#pragma unroll
      for (int m = 0; m < 4; m++) {
        const f32x4 a = acc[m][2 * t], c = acc[m][2 * t + 1];
        const u32x4 v = {pk_bf16(a.x, a.y), pk_bf16(a.z, a.w), pk_bf16(c.x, c.y), pk_bf16(c.z, c.w)};
        __builtin_amdgcn_raw_buffer_store_b128(v, rx, lo + (((part + t) & 3) * 3 + (t - 1)) * 16384 + m * 1024, 0, 16);
      }
    const int sl = s0 + 64 * part + wc * 32 + fr, dl = d0 + wr * 64 + q * 4;
    WAITV(0); BAR();
    u32x2 dk[4]; float bb[2]; unsigned stt[2];
#pragma unroll
    for (int m = 0; m < 4; m++) dk[m] = *(const LAS u32x2*)(unsigned long)(EB + 16384 + (wr * 64 + q * 4 + m * 16) * 2);
#pragma unroll
    for (int e = 0; e < 2; e++) {
      bb[e] = lo2f(*(const LAS u16*)(unsigned long)(EB + 16384 + 256 + (wc * 32 + e * 16 + fr) * 2));
      stt[e] = *(const LAS unsigned*)(unsigned long)(EB + 16384 + 512 + (wc * 32 + e * 16 + fr) * 4);
    }
#pragma unroll
    for (int e = 0; e < 2; e++) {
      const float mb = lo2f(stt[e]), rs = __builtin_amdgcn_rcpf(hi2f(stt[e]));
#pragma unroll
      for (int m = 0; m < 4; m++) {
        const unsigned a = EB + wr * 8192 + (wc * 32 + e * 16 + fr) * 128 + (((m * 2 + (q >> 1)) ^ (fr >> 1)) << 4) + (q & 1) * 8;
        const u32x2 xv = *(const LAS u32x2*)(unsigned long)a;
        const f32x4 xf = {lo2f(xv.x), hi2f(xv.x), lo2f(xv.y), hi2f(xv.y)};
        const unsigned n0 = cvt2(up2(cvt2(xf.xy - mb)) * rs), n1 = cvt2(up2(cvt2(xf.zw - mb)) * rs);
        const unsigned k0 = cvt2(up2(n0) * up2(dk[m].x)), k1 = cvt2(up2(n1) * up2(dk[m].y));
        acc[m][e] += xf + (f32x4){lo2f(k0), hi2f(k0), lo2f(k1), hi2f(k1)} + bb[e];
      }
    }
    BAR();
    const __amdgpu_buffer_rsrc_t ro = __builtin_amdgcn_make_buffer_rsrc(p.out, 0, S * D * 4, 0x00020000);
    const unsigned of = ((sl & ~8) * D + dl) * 4 + (fr >> 3) * 64;
    u32x4 rv[3][4];
#pragma unroll
    for (int m = 0; m < 4; m++)
#pragma unroll
      for (int t = 0; t < 3; t++)
        rv[t][m] = __builtin_amdgcn_raw_buffer_load_b128(rx, lo + m * 1024, (part * 3 + t) * 16384, 16);
#pragma unroll
    for (int m = 0; m < 4; m++)
#pragma unroll
      for (int t = 0; t < 3; t++) {
        const u32x4 v = rv[t][m];
        acc[m][0] += (f32x4){lo2f(v.x), hi2f(v.x), lo2f(v.y), hi2f(v.y)};
        acc[m][1] += (f32x4){lo2f(v.z), hi2f(v.z), lo2f(v.w), hi2f(v.w)};
        if (t == 2 && (m & 1)) {
#pragma unroll
          for (int e = 0; e < 2; e++) {
            const u32x4 A = __builtin_bit_cast(u32x4, acc[m - 1][e]), B = __builtin_bit_cast(u32x4, acc[m][e]);
            u32x4 lo_, hi_;
#pragma unroll
            for (int i = 0; i < 4; i++) {
              lo_[i] = __builtin_amdgcn_update_dpp(A[i], B[i], 0x128, 0xf, 0xc, false);
              hi_[i] = __builtin_amdgcn_update_dpp(B[i], A[i], 0x128, 0xf, 0x3, false);
            }
            __builtin_amdgcn_raw_buffer_store_b128(lo_, ro, of + e * 16 * D * 4 + (m - 1) * 64, 0, 16);
            __builtin_amdgcn_raw_buffer_store_b128(hi_, ro, of + (e * 16 + 8) * D * 4 + (m - 1) * 64, 0, 16);
          }
        }
      }
  }
}

__global__ __launch_bounds__(512, 2) void k_main(Ptrs p) {
  extern __shared__ __attribute__((aligned(16))) char shm[];
  const int wid = __builtin_amdgcn_readfirstlane(threadIdx.x >> 6);
  const int grp = wid >> 2, w4 = wid & 3, wr = w4 >> 1, wc = w4 & 1;
  unsigned long long pc0;
  asm volatile("s_getpc_b64 %0" : "=s"(pc0));
  if (blockIdx.x < 128) {
    const int j = blockIdx.x >> 3, xcd = blockIdx.x & 7;
    const int tf = xcd * 2 + (j >> 3), td = j & 7;
    const int f0 = tf * 224, fw = f0 & ~63, boff = (f0 - fw) >> 4;
    const unsigned ldR = F * 2, ldC = LDH * 2;
    const u32x4 rR = make_rsrc(p.w1 + fw);
    unsigned ep;
    {
      const int lane = lane_id();
      const int row = blockIdx.x * 16 + wid * 2;
      const u16* xr = p.x + (size_t)row * D + lane * 8;
      u32x4 a0 = *(const u32x4*)xr, a1 = *(const u32x4*)(xr + 512), a2 = *(const u32x4*)(xr + 1024), a3 = *(const u32x4*)(xr + 1536);
      const unsigned e0 = __hip_atomic_load((GAS unsigned*)p.epo_(), RLX_AGENT);
      ep = __builtin_amdgcn_readfirstlane(e0) + 1u;
      unsigned w[2][8] = {{a0.x, a0.y, a0.z, a0.w, a1.x, a1.y, a1.z, a1.w}, {a2.x, a2.y, a2.z, a2.w, a3.x, a3.y, a3.z, a3.w}}, st[2];
      ln2_rows<2>(w, st);
      a0 = (u32x4){w[0][0], w[0][1], w[0][2], w[0][3]}; a1 = (u32x4){w[0][4], w[0][5], w[0][6], w[0][7]};
      a2 = (u32x4){w[1][0], w[1][1], w[1][2], w[1][3]}; a3 = (u32x4){w[1][4], w[1][5], w[1][6], w[1][7]};
      const __amdgpu_buffer_rsrc_t rh = __builtin_amdgcn_make_buffer_rsrc(p.h_() + (size_t)row * LDH, 0, 0x7fffffff, 0x00020000);
      __builtin_amdgcn_raw_buffer_store_b128(a0, rh, lane * 16, 0, 16); __builtin_amdgcn_raw_buffer_store_b128(a1, rh, lane * 16 + 1024, 0, 16);
      __builtin_amdgcn_raw_buffer_store_b128(a2, rh, lane * 16 + ldC, 0, 16); __builtin_amdgcn_raw_buffer_store_b128(a3, rh, lane * 16 + 1024 + ldC, 0, 16);
      if (lane < 2) st_gran(p.stat_() + row + lane, ep, lane ? st[1] : st[0]);
      WAITV(0); BAR();
      if (wid == 0 && lane == 0) st_flag(p.lnf_() + blockIdx.x, ep);
    }
    if (grp == 1) {
      const int lane = lane_id();
      const u32x4 rC = make_rsrc(p.h_() + td * 128);
      const unsigned vR = (8 * w4 + (lane >> 3)) * ldR + swz_km(lane, w4 & 1), vC = (8 * w4 + (lane >> 3)) * ldC + swz_km(lane, w4 & 1);
      auto stage = [&](int kt, int buf, int i0, int i1) {
        const unsigned lb = buf * STG + w4 * 1024;
        const int kr = ((kt + 4 * td) & 31) * 64;
#pragma unroll
        for (int i = i0; i < i1; i++) {
          if (i < 8) dma16(rR, vR, kr * ldR + (32 * (i & 1) * ldR + (i >> 1) * 128), lb + i * 4096);
          else dma16c(rC, vC, kr * ldC + (32 * (i & 1) * ldC + ((i - 8) >> 1) * 128), lb + i * 4096);
        }
      };
      stage(0, 0, 0, 8);
      stage(1, 1, 0, 8);
      wait_words(p.lnf_() + 16 * td + 2 * (lane & 7), ep);
      stage(0, 0, 8, 12);
      stage(1, 1, 8, 12);
      WAITV(4); BAR();
      stage(2, 2, 0, 12);
      WAITV(12); BAR();
      stage(3, 0, 0, 12);
      producer_steady(1, S / 64, stage, [](int) { return 0; }, 4, [&](bool b) { return wait_words(p.lnf_() + 2 * lane, ep, b ? 0x10000u : 1u) || b; });
      BAR();
      {
        const __amdgpu_buffer_rsrc_t rg = __builtin_amdgcn_make_buffer_rsrc(p.g_() + (size_t)(td * 128) * LDG + f0, 0, 0x7fffffff, 0x00020000);
        unsigned la[4], ga[4];
#pragma unroll
        for (int i = 0; i < 4; i++) {
          const int id = w4 * 64 + lane + 256 * i, rr = (id * 2341) >> 16, c = id - rr * 28, r = (rr >> 4) * 64 + (rr & 15);
          la[i] = id < 896 ? r * GP + c * 16 : 0; ga[i] = id < 896 ? r * (LDG * 2) + c * 16 : 0x80000000u;
        }
#pragma unroll
        for (int n = 0; n < 4; n++) {
          BAR();
          u32x4 u[4];
#pragma unroll
          for (int i = 0; i < 4; i++) u[i] = *(const LAS u32x4*)(unsigned long)(la[i] + n * 16 * GP);
#pragma unroll
          for (int i = 0; i < 4; i++) __builtin_amdgcn_raw_buffer_store_b128(u[i], rg, ga[i] + n * 16 * (LDG * 2), 0, 16);
        }
      }
    } else {
      f32x4 acc[7][4];
      const int lane = lane_id();
      {
        const int br = blockIdx.x * 16 + w4 * 4;
        const u32x4* bp = (const u32x4*)((br < P ? p.Bre + (size_t)br * D : p.Bim + (size_t)(br - P) * D)) + lane;
        u32x4 t[8];
#pragma unroll
        for (int i = 0; i < 8; i++) t[i] = bp[i * 64];
        float sm[4];
#pragma unroll
        for (int r = 0; r < 4; r++) {
          float a = 0.f, c = 0.f;
#pragma unroll
          for (int i = 0; i < 4; i++) { a = dot2(t[2 * r][i], 0x3f803f80u, a); c = dot2(t[2 * r + 1][i], 0x3f803f80u, c); }
          sm[r] = wave_sum(a + c);
        }
        if (lane < 4) st_gran(p.sb_() + br + lane, ep, __float_as_uint(lane == 0 ? sm[0] : lane == 1 ? sm[1] : lane == 2 ? sm[2] : sm[3]));
      }
      const int fb = f0 + wr * 112 + (lane >> 4) * 4;
#pragma unroll
      for (int m = 0; m < 7; m++) {
        const u32x2 bb = *(const u32x2*)(p.b1 + fb + m * 16);
        const f32x4 bv = {lo2f(bb.x), hi2f(bb.x), lo2f(bb.y), hi2f(bb.y)};
#pragma unroll
        for (int n = 0; n < 4; n++) acc[m][n] = bv;
      }
      consumer_loop<1, 1, 7, 4, 31 % NS>(acc, 31 / NS, lane, boff + wr * 7, wc * 4, 256 * 128);
      BAR();
      const int ln = lane_id();
      const unsigned wa = (wc * 64 + (ln & 15)) * GP + (wr * 112 + (ln >> 4) * 4) * 2;
#pragma unroll
      for (int n = 0; n < 4; n++) {
#pragma unroll
        for (int m = 0; m < 7; m++) {
          const f32x4 u = acc[m][n];
          u32x2 o; o.x = cvt2(gelu2(u.xy)); o.y = cvt2(gelu2(u.zw));
          *(LAS u32x2*)(unsigned long)(wa + n * 16 * GP + m * 32) = o;
        }
        WAITL0(); BAR();
      }
    }
    WAITV(0); BAR();
    if (wid == 0 && lane_id() == 0) st_flag(p.gfl_() + td * 16 + tf, ep);
    phase2<1>(p, wid, j, xcd, ep);
  } else {
    const int j = (blockIdx.x >> 3) - 16, xcd = blockIdx.x & 7;
    const int tl = (xcd & 1) * 4 + (j & 3), tp = (xcd >> 1) * 4 + (j >> 2);
    const int l0 = tl * 256, p0 = tp * 64;
    auto hs_out = [&](int t, int nthr, int n, int c) {
      const __amdgpu_buffer_rsrc_t rh = __builtin_amdgcn_make_buffer_rsrc(p.hsT_() + (size_t)p0 * LDS_ + l0, 0, 0x7fffffff, 0x00020000);
#pragma unroll
      for (int i = 0; i < n; i++) {
        const int rr = (t >> 5) + (nthr >> 5) * i, r = (rr >> 4) * 32 + c * 16 + (rr & 15), ck = t & 31;
        const u32x4 u = *(const LAS u32x4*)(unsigned long)(r * HP + ck * 16);
        __builtin_amdgcn_raw_buffer_store_b128(u, rh, ((r & 63) + (r >> 6) * P) * (LDS_ * 2) + ck * 16, 0, 16);
      }
    };
    if (grp == 1) {
      const int lane = lane_id();
      const u32x4 rR = make_rsrc(p.x + (size_t)l0 * D), rC = make_rsrc(((w4 >> 1) ? p.Bim : p.Bre) + (size_t)p0 * D);
      const unsigned ld = D * 2; const int r8 = lane >> 3;
      const unsigned vR = (32 * (2 * (w4 & 1) + (r8 >> 2)) + 16 * (r8 & 1) + ((r8 >> 1) & 1) + 2 * (w4 >> 1)) * ld + swz_kc(lane, w4 & 1);
      const unsigned vC = (8 * (w4 & 1) + r8) * ld + swz_kc(lane, w4 & 1);
      const int a = (xcd * 16 + j) * 4 + w4;
      const u32x4 rT = make_rsrc(w4 < 2 ? p.stat_() + l0 + w4 * 128 : p.sb_() + (w4 - 2) * P + p0);
      if (w4 == 0) dma4c(make_rsrc(p.epo_()), 0, 0, L_EP);
      producer_loop(D / 64, [&](int kt, int buf, int i0, int i1) {
        const unsigned lb = buf * STG + w4 * 1024;
        const int kr = ((kt + 2 * tl) & 15) * 128;
#pragma unroll
        for (int i = i0; i < i1; i++) {
          if (i < 8) dma16(rR, vR, kr + ((i >> 2) * 128 + 4 * (i & 3)) * ld, lb + i * 4096);
          else dma16(rC, vC, kr + 16 * (i - 8) * ld, lb + i * 4096);
        }
      }, [&](int k) {
        if (k == 12) { dma16c(rT, lane * 16, 0, L_RAW + w4 * 1024); touch(make_rsrc((a & 1) ? p.Cim : p.Cre), lane * 128, (a >> 1) * 8192, L_DUMMY); return 2; }
        if (k == 13 || (k == 14 && a < 256)) { touch(make_rsrc(p.w2), lane * 128, w2_chunk((k - 13) * 512 + a) * 8192, L_DUMMY); return 1; }
        return 0;
      }, -1, [](bool) { return true; });
      const unsigned ep = lds_epoch();
      {
        if (a < 256) { WAITV(3); } else { WAITV(2); }
        u32x4 gq = *(const volatile LAS u32x4*)(unsigned long)(L_RAW + w4 * 1024 + lane * 16);
        const bool need = w4 < 2 || lane < 32;
        for (unsigned spin = 0; spin < SPIN; spin++) {
          if (__builtin_amdgcn_ballot_w64(need && (gq.y != ep || gq.w != ep)) == 0ull) break;
          __builtin_amdgcn_s_sleep(4);
          dma16c(rT, lane * 16, 0, L_RAW + w4 * 1024);
          WAITV(0);
          gq = *(const volatile LAS u32x4*)(unsigned long)(L_RAW + w4 * 1024 + lane * 16);
        }
        if (w4 < 2) {
          const int t = 2 * lane, idx = ((w4 * 4 + (t >> 5)) * 8 + ((t & 15) >> 1)) * 4 + ((t >> 4) & 1);
          const float r0 = __builtin_amdgcn_rcpf(hi2f(gq.x)), r1 = __builtin_amdgcn_rcpf(hi2f(gq.z));
          LAS float* rs = (LAS float*)(unsigned long)L_RS;
          rs[idx] = r0; rs[idx + 2] = r1; rs[256 + idx] = -lo2f(gq.x) * r0; rs[256 + idx + 2] = -lo2f(gq.z) * r1;
        } else if (lane < 32) {
          *(LAS u32x2*)(unsigned long)(L_SB + (w4 - 2) * 256 + lane * 8) = (u32x2){gq.x, gq.z};
        }
      }
      WAITL0(); BAR();
      BAR();
      if (w4 < 2) {
        const int q = lane >> 4, ch0 = p0 + w4 * 32 + (lane & 15);
        f32x4 res = {0.f, 0.f, 0.f, 0.f};
        if (tl > 0) {
          cf l256[2];
#pragma unroll
          for (int c = 0; c < 2; c++) { const f32x2 t = *(const LAS f32x2*)(unsigned long)(L_CF + (w4 * 32 + c * 16 + (lane & 15)) * 64 + 48); l256[c] = cf{t.x, t.y}; }
          float gv[2][2][2];
          for (unsigned spin = 0; spin < SPIN; spin++) {
            const int te = (lane & 7) < tl ? (lane & 7) : 0;
            const u64 w = __hip_atomic_load((GAS u64*)p.gran_() + ((size_t)te * P + p0 + w4 * 32) * 2, RLX_AGENT);
            if (__builtin_amdgcn_ballot_w64((unsigned)(w >> 32) != ep) == 0ull) break;
            __builtin_amdgcn_s_sleep(2);
          }
          for (unsigned spin = 0; spin < SPIN; spin++) {
            bool ok = true;
#pragma unroll
            for (int t = 0; t < 2; t++)
#pragma unroll
              for (int c = 0; c < 2; c++)
#pragma unroll
                for (int pt = 0; pt < 2; pt++) {
                  const int tt = q + 4 * t, te = tt < tl ? tt : 0;
                  const u64 w = __hip_atomic_load((GAS u64*)p.gran_() + ((size_t)te * P + ch0 + 16 * c) * 2 + pt, RLX_AGENT);
                  gv[t][c][pt] = __uint_as_float((unsigned)w); ok &= (unsigned)(w >> 32) == ep;
                }
            if (__builtin_amdgcn_ballot_w64(!ok) == 0ull) break;
            __builtin_amdgcn_s_sleep(2);
          }
#pragma unroll
          for (int c = 0; c < 2; c++) {
            const cf l512 = cmul(l256[c], l256[c]), l1024 = cmul(l512, l512);
            cf sum{0.f, 0.f};
#pragma unroll
            for (int t = 0; t < 2; t++) {
              const int tt = q + 4 * t, ex = tl - 1 - tt;
              cf v{gv[t][c][0], gv[t][c][1]};
              if (ex & 1) v = cmul(v, l256[c]);
              if (ex & 2) v = cmul(v, l512);
              if (ex & 4) v = cmul(v, l1024);
              if (ex >= 0) { sum.r += v.r; sum.i += v.i; }
            }
            sum.r += __shfl_xor(sum.r, 16); sum.i += __shfl_xor(sum.i, 16);
            sum.r += __shfl_xor(sum.r, 32); sum.i += __shfl_xor(sum.i, 32);
            res[2 * c] = sum.r; res[2 * c + 1] = sum.i;
          }
        }
        if (q == 0) ((LAS f32x4*)(unsigned long)L_TIN)[w4 * 16 + (lane & 15)] = res;
      }
      WAITL0(); BAR();
      BAR();
      hs_out(w4 * 64 + lane, 256, 8, 0);
    } else {
      warm_code(pc0, j * 4 + w4, lane_id());
      if (w4 == 1) {
        const int ln = lane_id(), ch = p0 + ln;
        const float lr = lo2f(p.lre[ch]), li = lo2f(p.lim[ch]), dt = rbf(expf(lo2f(p.ldt[ch])));
        const float er = expf(dt * lr); float sn, cs; sincosf(dt * li, &sn, &cs);
        const float ar = er * cs, ai = er * sn, nr = ar - 1.f, ni = ai, den = lr * lr + li * li;
        cf t{ar, ai};
#pragma unroll
        for (int i = 0; i < 4; i++) t = cmul(t, t);
        const cf l32 = cmul(t, t), l64 = cmul(l32, l32), l128 = cmul(l64, l64), l256 = cmul(l128, l128);
        LAS f32x4* o = (LAS f32x4*)(unsigned long)(L_CF + ln * 64);
        o[0] = (f32x4){ar, ai, 2.f * ((nr * lr + ni * li) / den), 2.f * ((ni * lr - nr * li) / den)};
        o[1] = (f32x4){t.r, t.i, l32.r, l32.i}; o[2] = (f32x4){l64.r, l64.i, l128.r, l128.i}; o[3] = (f32x4){l256.r, l256.i, 0.f, 0.f};
      }
      f32x4 acc[8][4];
#pragma unroll
      for (int m = 0; m < 8; m++)
#pragma unroll
        for (int n = 0; n < 4; n++) acc[m][n] = (f32x4){0.f, 0.f, 0.f, 0.f};
      consumer_loop<0, 0, 8, 4, 15 % NS>(acc, 15 / NS, lane_id(), wr * 8, wc * 4, 256 * 128);
      BAR();
      const int lane = lane_id();
      const int q = lane >> 4, fr = lane & 15, ch0 = p0 + wc * 32 + fr;
      f32x4 ct[2][3];
#pragma unroll
      for (int c = 0; c < 2; c++)
#pragma unroll
        for (int i = 0; i < 3; i++) ct[c][i] = *(const LAS f32x4*)(unsigned long)(L_CF + (wc * 32 + c * 16 + fr) * 64 + i * 16);
      float sb[4];
#pragma unroll
      for (int n = 0; n < 4; n++) sb[n] = *(const LAS float*)(unsigned long)(L_SB + (n & 1) * 256 + (wc * 32 + (n >> 1) * 16 + fr) * 4);
      f32x2 er[2] = {{0.f, 0.f}, {0.f, 0.f}}, ei[2] = {{0.f, 0.f}, {0.f, 0.f}};
#pragma unroll
      for (int m = 0; m < 8; m++) {
        const f32x4 rs = *(const LAS f32x4*)(unsigned long)(L_RS + ((wr * 4 + q) * 8 + m) * 16), ms = *(const LAS f32x4*)(unsigned long)(L_RS + 1024 + ((wr * 4 + q) * 8 + m) * 16);
#pragma unroll
        for (int n = 0; n < 4; n++) acc[m][n] = acc[m][n] * rs + ms * sb[n];
#pragma unroll
        for (int hh = 0; hh < 2; hh++)
#pragma unroll
          for (int c = 0; c < 2; c++) {
            const f32x2 ur = hh ? acc[m][2 * c].zw : acc[m][2 * c].xy, ui = hh ? acc[m][2 * c + 1].zw : acc[m][2 * c + 1].xy;
            const f32x2 nr = er[c] * ct[c][0].x + (ur - ei[c] * ct[c][0].y), ni = ei[c] * ct[c][0].x + (ui + er[c] * ct[c][0].y);
            er[c] = nr; ei[c] = ni;
          }
      }
      cf cin[2], wagg[2];
#pragma unroll
      for (int c = 0; c < 2; c++) {
        const cf l16{ct[c][1].x, ct[c][1].y}, l32{ct[c][1].z, ct[c][1].w}, l64{ct[c][2].x, ct[c][2].y};
        cf s = cfma(l16, cf{er[c].x, ei[c].x}, cf{er[c].y, ei[c].y});
        cf u1{__shfl_up(s.r, 16), __shfl_up(s.i, 16)};
        if (q >= 1) s = cfma(l32, u1, s);
        cf u2{__shfl_up(s.r, 32), __shfl_up(s.i, 32)};
        if (q >= 2) s = cfma(l64, u2, s);
        wagg[c] = s;
        cf ex{__shfl_up(s.r, 16), __shfl_up(s.i, 16)};
        cin[c] = q >= 1 ? ex : cf{0.f, 0.f};
      }
      LAS f32x4* ag = (LAS f32x4*)(unsigned long)L_AG;
      if (wr == 0 && q == 3) ag[wc * 16 + fr] = (f32x4){wagg[0].r, wagg[0].i, wagg[1].r, wagg[1].i};
      WAITL0(); BAR();
      const unsigned ep = lds_epoch();
      cf w0[2];
      { const f32x4 t = ag[wc * 16 + fr]; w0[0] = cf{t.x, t.y}; w0[1] = cf{t.z, t.w}; }
      if (wr == 1 && tl < 7 && q == 3) {
#pragma unroll
        for (int c = 0; c < 2; c++) {
          const cf t = cfma(cf{ct[c][2].z, ct[c][2].w}, w0[c], wagg[c]);
          u64* gp = p.gran_() + ((size_t)tl * P + ch0 + 16 * c) * 2;
          st_gran(gp, ep, __float_as_uint(t.r)); st_gran(gp + 1, ep, __float_as_uint(t.i));
        }
      }
#pragma unroll
      for (int c = 0; c < 2; c++) {
        const float cr = ct[c][0].z, ci = ct[c][0].w;
#pragma unroll
        for (int m = 0; m < 8; m++) {
          const f32x4 ur = acc[m][2 * c], ui = acc[m][2 * c + 1];
          acc[m][2 * c] = ur * cr - ui * ci; acc[m][2 * c + 1] = ui * (-cr) - ur * ci;
          asm volatile("" : "+v"(acc[m][2 * c]), "+v"(acc[m][2 * c + 1]));
        }
      }
      BAR();
      const f32x4 tv = ((const LAS f32x4*)(unsigned long)L_TIN)[wc * 16 + fr];
      f32x2 orr[2], oi[2];
#pragma unroll
      for (int c = 0; c < 2; c++) {
        const cf tin{tv[2 * c], tv[2 * c + 1]};
        cf win = wr == 1 ? cfma(cf{ct[c][2].z, ct[c][2].w}, tin, w0[c]) : tin;
        if (q & 1) win = cmul(win, cf{ct[c][1].z, ct[c][1].w});
        if (q & 2) win = cmul(win, cf{ct[c][2].x, ct[c][2].y});
        const cf ha{cin[c].r + win.r, cin[c].i + win.i};
        const cf hb = cfma(cf{ct[c][1].x, ct[c][1].y}, ha, cf{er[c].x, ei[c].x});
        const cf c2{ct[c][0].z, ct[c][0].w}, oa = cmul(c2, ha), ob = cmul(c2, hb);
        orr[c] = (f32x2){oa.r, ob.r}; oi[c] = (f32x2){-oa.i, -ob.i};
      }
      const unsigned wa = (wc * 32 + fr) * HP + (wr * 128 + 32 * q) * 2;
#pragma unroll
      for (int c = 0; c < 2; c++) {
        const float lr = ct[c][0].x, li = ct[c][0].y;
#pragma unroll
        for (int mp = 0; mp < 2; mp++) {
          u32x4 o[2][2];
#pragma unroll
          for (int mm = 0; mm < 4; mm++) {
            const int m = 4 * mp + mm;
            const f32x2 r0 = orr[c] * lr + (acc[m][2 * c].xy + oi[c] * li), i0 = oi[c] * lr + (acc[m][2 * c + 1].xy - orr[c] * li);
            const f32x2 r1 = r0 * lr + (acc[m][2 * c].zw + i0 * li), i1 = i0 * lr + (acc[m][2 * c + 1].zw - r0 * li);
            orr[c] = r1; oi[c] = i1;
            o[0][0][mm] = pk_bf16(r0.x, r1.x); o[0][1][mm] = pk_bf16(r0.y, r1.y);
            o[1][0][mm] = pk_bf16(i0.x, i1.x); o[1][1][mm] = pk_bf16(i0.y, i1.y);
          }
#pragma unroll
          for (int pt = 0; pt < 2; pt++)
#pragma unroll
            for (int sg = 0; sg < 2; sg++) *(LAS u32x4*)(unsigned long)(wa + (pt * 64 + c * 16) * HP + sg * 32 + mp * 16) = o[pt][sg];
        }
        if (c == 0) { WAITL0(); BAR(); }
      }
    }
    WAITL0(); BAR();
    hs_out(wid * 64 + lane_id(), 512, 4, 1);
    const unsigned ep = lds_epoch();
    WAITV(0); BAR();
    if (wid == 0 && lane_id() == 0) st_flag(p.gfl_() + 128 + tl * 16 + tp, ep);
    phase2<0>(p, wid, j, xcd, ep);
  }
}

extern "C" void kernel_launch(void* const* d_in, const int* in_sizes, int n_in, void* d_out, int out_size, void* d_ws, size_t ws_size,
                              hipStream_t stream) {
  Ptrs p;
  p.x = (const u16*)d_in[0]; p.lre = (const u16*)d_in[1]; p.lim = (const u16*)d_in[2];
  p.Bre = (const u16*)d_in[3]; p.Bim = (const u16*)d_in[4]; p.Cre = (const u16*)d_in[5]; p.Cim = (const u16*)d_in[6];
  p.ldt = (const u16*)d_in[7]; p.dsk = (const u16*)d_in[8];
  p.w1 = (const u16*)d_in[10]; p.b1 = (const u16*)d_in[11]; p.w2 = (const u16*)d_in[12]; p.b2 = (const u16*)d_in[13];
  p.ws = (char*)d_ws;
  p.out = (float*)d_out;
  (void)hipFuncSetAttribute((const void*)k_main, hipFuncAttributeMaxDynamicSharedMemorySize, L_TOT);
  k_main<<<256, 512, L_TOT, stream>>>(p);
}
```
